```python
import torch

def _softmax(x, axis=-1):
    return torch.softmax(x, dim=axis)
def _gelu(x):
    return torch.nn.functional.gelu(x, approximate="tanh")
def _silu(x):
    return torch.nn.functional.silu(x)
def _sigmoid(x):
    return torch.sigmoid(x)
def _layernorm(x, axis=-1, eps=1e-5):
    m = x.mean(dim=axis, keepdim=True)
    v = x.var(dim=axis, keepdim=True, unbiased=False)
    return (x - m) / torch.sqrt(v + eps)
def _scatter_add(tgt, idx, src, axis=0):
    return tgt.clone().index_add_(axis, idx, src)
def _segment_sum(src, seg, n):
    out = torch.zeros((n,) + src.shape[1:], dtype=src.dtype)
    return out.index_add_(0, seg, src)
def _pad(x, pw):
    flat = tuple(v for pair in reversed(pw) for v in pair)
    return torch.nn.functional.pad(x, flat)

kernel_name = 'standalone__mha_block_cached__near_ridge'

def setup_inputs():

    _w = (lambda a: (lambda t: t.to(torch.bfloat16) if t.is_floating_point() else t)(torch.as_tensor(a)))
    B, Sq, D = 16, 32, 1024
    H, Sk, Dh = 32, 32, 128
    return {
        "x": _w((lambda *s: torch.randn(*s, dtype=torch.bfloat16) * 0.8)(B, Sq, D)),
        "k_cache": _w((lambda *s: torch.randn(*s, dtype=torch.bfloat16) * 0.8)(B, H, Sk, Dh)),
        "v_cache": _w((lambda *s: torch.randn(*s, dtype=torch.bfloat16) * 0.05)(B, H, Sk, Dh)),
        "wq": _w((lambda *s: torch.randn(*s, dtype=torch.bfloat16) * 0.8)(D, H * Dh) * (D ** -0.5)),
        "wo": _w((lambda *s: torch.randn(*s, dtype=torch.bfloat16) * 0.05)(H * Dh, D) * ((H * Dh) ** -0.5)),
    }

def reference(x, k_cache, v_cache, wq, wo):
    B, Sq, D = x.shape
    H, Dh = 32, 128
    q = (lambda x, *d: x.permute(*d))((x @ wq).reshape(B, Sq, H, Dh), 0, 2, 1, 3)
    scores = torch.einsum("bhqd,bhkd->bhqk", q, k_cache) * (Dh ** -0.5)
    p = _softmax(scores, axis=-1)
    o = torch.einsum("bhqk,bhkd->bhqd", p, v_cache)
    return (lambda x, *d: x.permute(*d))(o, 0, 2, 1, 3).reshape(B, Sq, H * Dh) @ wo
```

```cpp
#include <hip/hip_runtime.h>
#include <hip/hip_bf16.h>
#include <stdint.h>

typedef short bf16x8 __attribute__((ext_vector_type(8)));
typedef float f32x4 __attribute__((ext_vector_type(4)));
typedef unsigned u32x4 __attribute__((ext_vector_type(4)));
typedef unsigned u32x2 __attribute__((ext_vector_type(2)));

#define DEV __device__ __forceinline__

static constexpr int B_ = 16, SQ = 32, D_ = 1024, H_ = 32, SK = 32, DH = 128;
static constexpr int M_ = B_ * SQ;
static constexpr int NQ = H_ * DH;

DEV unsigned pack_bf16(float lo, float hi) {
    __hip_bfloat16 a = __float2bfloat16(lo), b = __float2bfloat16(hi);
    return (unsigned)__builtin_bit_cast(unsigned short, a) | ((unsigned)__builtin_bit_cast(unsigned short, b) << 16);
}

__global__ void __launch_bounds__(256) k1_naive(const __hip_bfloat16* __restrict__ x, const __hip_bfloat16* __restrict__ kc,
                         const __hip_bfloat16* __restrict__ vc, const __hip_bfloat16* __restrict__ wq,
                         __hip_bfloat16* __restrict__ o) {
    __shared__ float q[SQ][DH + 1];
    __shared__ float s[SQ][SK + 1];
    int b = blockIdx.x / H_, h = blockIdx.x % H_;
    int t = threadIdx.x;
    for (int idx = t; idx < SQ * DH; idx += 256) {
        int r = idx / DH, c = idx % DH;
        float acc = 0.f;
        const __hip_bfloat16* xr = x + (size_t)(b * SQ + r) * D_;
        for (int k = 0; k < D_; ++k) acc += __bfloat162float(xr[k]) * __bfloat162float(wq[(size_t)k * NQ + h * DH + c]);
        q[r][c] = __bfloat162float(__float2bfloat16(acc));
    }
    __syncthreads();
    const __hip_bfloat16* kb = kc + (size_t)(b * H_ + h) * SK * DH;
    const __hip_bfloat16* vb = vc + (size_t)(b * H_ + h) * SK * DH;
    for (int idx = t; idx < SQ * SK; idx += 256) {
        int r = idx / SK, c = idx % SK;
        float acc = 0.f;
        for (int d = 0; d < DH; ++d) acc += q[r][d] * __bfloat162float(kb[c * DH + d]);
        s[r][c] = acc * 0.08838834764831845f;
    }
    __syncthreads();
    if (t < SQ) {
        float m = -1e30f;
        for (int c = 0; c < SK; ++c) m = fmaxf(m, s[t][c]);
        float sum = 0.f;
        for (int c = 0; c < SK; ++c) { float e = __expf(s[t][c] - m); s[t][c] = e; sum += e; }
        float inv = 1.f / sum;
        for (int c = 0; c < SK; ++c) s[t][c] = __bfloat162float(__float2bfloat16(s[t][c] * inv));
    }
    __syncthreads();
    for (int idx = t; idx < SQ * DH; idx += 256) {
        int r = idx / DH, c = idx % DH;
        float acc = 0.f;
        for (int k = 0; k < SK; ++k) acc += s[r][k] * __bfloat162float(vb[k * DH + c]);
        o[(size_t)(b * SQ + r) * NQ + h * DH + c] = __float2bfloat16(acc);
    }
}

__global__ void __launch_bounds__(256) k2_naive(const __hip_bfloat16* __restrict__ o, const __hip_bfloat16* __restrict__ wo,
                         __hip_bfloat16* __restrict__ out) {
    int idx = blockIdx.x * 256 + threadIdx.x;
    int r = idx / D_, c = idx % D_;
    float acc = 0.f;
    const __hip_bfloat16* orow = o + (size_t)r * NQ;
    for (int k = 0; k < NQ; ++k) acc += __bfloat162float(orow[k]) * __bfloat162float(wo[(size_t)k * D_ + c]);
    out[idx] = __float2bfloat16(acc);
}

extern "C" void kernel_launch(void* const* d_in, const int* in_sizes, int n_in,
                              void* d_out, int out_size, void* d_ws, size_t ws_size,
                              hipStream_t stream) {
    const __hip_bfloat16* x = (const __hip_bfloat16*)d_in[0];
    const __hip_bfloat16* kc = (const __hip_bfloat16*)d_in[1];
    const __hip_bfloat16* vc = (const __hip_bfloat16*)d_in[2];
    const __hip_bfloat16* wq = (const __hip_bfloat16*)d_in[3];
    const __hip_bfloat16* wo = (const __hip_bfloat16*)d_in[4];
    __hip_bfloat16* out = (__hip_bfloat16*)d_out;
    __hip_bfloat16* o = (__hip_bfloat16*)d_ws;
    k1_naive<<<B_ * H_, 256, 0, stream>>>(x, kc, vc, wq, o);
    k2_naive<<<M_ * D_ / 256, 256, 0, stream>>>(o, wo, out);
}
```

```cpp
#include <hip/hip_runtime.h>
#include <hip/hip_bf16.h>
#include <stdint.h>

typedef short bf16x8 __attribute__((ext_vector_type(8)));
typedef float f32x4 __attribute__((ext_vector_type(4)));
typedef unsigned u32x4 __attribute__((ext_vector_type(4)));
typedef unsigned u32x2 __attribute__((ext_vector_type(2)));

#define DEV __device__ __forceinline__

static constexpr int B_ = 16, SQ = 32, D_ = 1024, H_ = 32, SK = 32, DH = 128;
static constexpr int M_ = B_ * SQ;
static constexpr int NQ = H_ * DH;

DEV unsigned cvt_pk(float lo, float hi) {
    unsigned r;
    asm("v_cvt_pk_bf16_f32 %0, %1, %2" : "=v"(r) : "v"(lo), "v"(hi));
    return r;
}
DEV unsigned perm_lo(unsigned a, unsigned b) { return __builtin_amdgcn_perm(b, a, 0x05040100u); }
DEV unsigned perm_hi(unsigned a, unsigned b) { return __builtin_amdgcn_perm(b, a, 0x07060302u); }

DEV void gload4(u32x4& d, unsigned voff, const void* sbase) {
    asm volatile("global_load_dwordx4 %0, %1, %2" : "=v"(d) : "v"(voff), "s"(sbase) : "memory");
}
DEV void gload2(u32x2& d, unsigned voff, const void* sbase) {
    asm volatile("global_load_dwordx2 %0, %1, %2" : "=v"(d) : "v"(voff), "s"(sbase) : "memory");
}
DEV void glds16(unsigned voff, const void* sbase, unsigned lds_addr) {
    unsigned keep;
    asm volatile("s_mov_b32 %0, m0\n\ts_mov_b32 m0, %3\n\ts_nop 0\n\tglobal_load_lds_dwordx4 %1, %2\n\ts_mov_b32 m0, %0"
                 : "=&s"(keep) : "v"(voff), "s"(sbase), "s"(lds_addr) : "memory");
}
template <int N> DEV void wait_vm() { asm volatile("s_waitcnt vmcnt(%0)" ::"i"(N) : "memory"); }
template <int N> DEV void wait_vm8(u32x4* w) {
    asm volatile("s_waitcnt vmcnt(%8)"
                 : "+v"(w[0]), "+v"(w[1]), "+v"(w[2]), "+v"(w[3]), "+v"(w[4]), "+v"(w[5]), "+v"(w[6]), "+v"(w[7])
                 : "i"(N) : "memory");
}

DEV bf16x8 as_frag(u32x4 v) { return __builtin_bit_cast(bf16x8, v); }
DEV f32x4 mfma16(bf16x8 a, bf16x8 b, f32x4 c) { return __builtin_amdgcn_mfma_f32_16x16x32_bf16(a, b, c, 0, 0, 0); }

DEV void transpose8x8(const u32x4* in, u32x4* out) {
#pragma unroll
    for (int u = 0; u < 4; ++u) {
#pragma unroll
        for (int p = 0; p < 4; ++p) {
            out[2 * u][p] = perm_lo(in[2 * p][u], in[2 * p + 1][u]);
            out[2 * u + 1][p] = perm_hi(in[2 * p][u], in[2 * p + 1][u]);
        }
    }
}

__global__ void __launch_bounds__(256, 1)
k_qattn(const __hip_bfloat16* __restrict__ x, const __hip_bfloat16* __restrict__ kc,
        const __hip_bfloat16* __restrict__ vc, const __hip_bfloat16* __restrict__ wq,
        unsigned char* __restrict__ ofrag) {
    extern __shared__ __attribute__((aligned(16))) unsigned char lds[];
    const int tid = threadIdx.x;
    const int w = __builtin_amdgcn_readfirstlane(tid >> 6);
    const int l = tid & 63, c = l & 15, g = l >> 4;
    const int bid = blockIdx.x;
    const int xcd = bid & 7, idx = bid >> 3;
    const int h = xcd * 4 + (idx & 3);
    const int bp = idx >> 2;
    const int m0 = bp * 64;

    const unsigned lds_base = (unsigned)(size_t)lds;
    const unsigned wbase = lds_base + w * 32768;

    const unsigned xoff_e = ((l >> 3) * 1024 + (((l & 7) ^ (l >> 4)) * 8)) * 2;
    const unsigned xoff_o = ((l >> 3) * 1024 + (((l & 7) ^ (l >> 4) ^ 4) * 8)) * 2;
    const unsigned char* xb = (const unsigned char*)x + ((size_t)m0 * 1024 + 256 * w) * 2;
    const unsigned woff = (8 * g * 4096 + 8 * c) * 2;
    const unsigned char* wb = (const unsigned char*)wq + ((size_t)(256 * w) * 4096 + h * 128) * 2;

    auto issue_x = [&](int kp) {
#pragma unroll
        for (int i = 0; i < 8; ++i)
            glds16((i & 1) ? xoff_o : xoff_e, xb + (size_t)i * 8 * 2048 + kp * 128, wbase + kp * 8192 + i * 1024);
    };
    u32x4 W[4][8];
    auto issue_w = [&](int s) {
#pragma unroll
        for (int j = 0; j < 8; ++j) gload4(W[s & 3][j], woff, wb + (size_t)(32 * s + j) * 8192);
    };

    f32x4 acc[8][4];
#pragma unroll
    for (int t = 0; t < 8; ++t)
#pragma unroll
        for (int r = 0; r < 4; ++r) acc[t][r] = f32x4{0.f, 0.f, 0.f, 0.f};

    issue_x(0); issue_w(0); issue_w(1); issue_x(1); issue_w(2); issue_w(3); issue_x(2);

    u32x4 KF[8], VF[8];
    const int b = 2 * bp + (w >> 1);
    const unsigned char* kbase = (const unsigned char*)kc + (size_t)(b * 32 + h) * 32 * 128 * 2;
    const unsigned char* vbase = (const unsigned char*)vc + (size_t)(b * 32 + h) * 32 * 128 * 2;
    const unsigned koff = (c * 128 + 32 * g) * 2;
    const unsigned voff = (4 * g * 128 + 8 * c) * 2;

    const unsigned char* xl0 = lds + w * 32768 + c * 128 + ((g ^ (c >> 1)) & 7) * 16;
    const unsigned char* xl1 = lds + w * 32768 + c * 128 + ((g ^ (c >> 1) ^ 4) & 7) * 16;
    int rboff[4];
#pragma unroll
    for (int r = 0; r < 4; ++r) rboff[r] = ((r + w) & 3) * 2048;

#pragma unroll
    for (int s = 0; s < 8; ++s) {
        if (s == 0) wait_vm8<40>(W[0]);
        if (s == 1) wait_vm8<48>(W[1]);
        if (s == 2) wait_vm8<40>(W[2]);
        if (s == 3) wait_vm8<40>(W[3]);
        if (s == 4) wait_vm8<24>(W[0]);
        if (s == 5) wait_vm8<24>(W[1]);
        if (s == 6) wait_vm8<24>(W[2]);
        if (s == 7) wait_vm8<16>(W[3]);
        u32x4 A[8];
        transpose8x8(W[s & 3], A);
        bf16x8 XB[4];
        const unsigned char* xl = (s & 1) ? xl1 : xl0;
#pragma unroll
        for (int r = 0; r < 4; ++r) XB[r] = *(const bf16x8*)(xl + (s >> 1) * 8192 + rboff[r]);
        __builtin_amdgcn_sched_barrier(0);
        if (s == 0) issue_x(3);
        if (s < 4) issue_w(s + 4);
        if (s == 4) {
#pragma unroll
            for (int i = 0; i < 8; ++i) gload4(KF[i], koff, kbase + (i >> 2) * 16 * 256 + (i & 3) * 16);
        }
        if (s == 5) {
#pragma unroll
            for (int j = 0; j < 8; ++j) gload4(VF[j], voff, vbase + ((j < 4) ? j : 12 + j) * 256);
        }
        __builtin_amdgcn_sched_barrier(0);
#pragma unroll
        for (int t = 0; t < 8; ++t)
#pragma unroll
            for (int r = 0; r < 4; ++r) acc[t][r] = mfma16(as_frag(A[t]), XB[r], acc[t][r]);
        __builtin_amdgcn_sched_barrier(0);
    }

#pragma unroll
    for (int r = 1; r < 4; ++r) {
        unsigned char* p = lds + w * 32768 + ((r + w) & 3) * 8192 + l * 16;
#pragma unroll
        for (int t = 0; t < 8; ++t) *(f32x4*)(p + t * 1024) = acc[t][r];
    }
    __syncthreads();
#pragma unroll
    for (int r = 1; r < 4; ++r) {
        const unsigned char* p = lds + ((w + r) & 3) * 32768 + w * 8192 + l * 16;
#pragma unroll
        for (int t = 0; t < 8; ++t) acc[t][0] += *(const f32x4*)(p + t * 1024);
    }

    u32x4 QF[4];
#pragma unroll
    for (int sp = 0; sp < 4; ++sp)
#pragma unroll
        for (int p = 0; p < 4; ++p) QF[sp][p] = cvt_pk(acc[2 * p][0][sp], acc[2 * p + 1][0][sp]);

    wait_vm8<8>(KF);
    f32x4 sacc[2];
#pragma unroll
    for (int kb = 0; kb < 2; ++kb) {
        sacc[kb] = f32x4{0.f, 0.f, 0.f, 0.f};
#pragma unroll
        for (int sp = 0; sp < 4; ++sp) sacc[kb] = mfma16(as_frag(KF[kb * 4 + sp]), as_frag(QF[sp]), sacc[kb]);
    }
    const float sc = 0.08838834764831845f * 1.4426950408889634f;
    float v[8];
#pragma unroll
    for (int i = 0; i < 8; ++i) v[i] = sacc[i >> 2][i & 3] * sc;
    float mx = v[0];
#pragma unroll
    for (int i = 1; i < 8; ++i) mx = fmaxf(mx, v[i]);
    mx = fmaxf(mx, __shfl_xor(mx, 16));
    mx = fmaxf(mx, __shfl_xor(mx, 32));
    float sum = 0.f;
#pragma unroll
    for (int i = 0; i < 8; ++i) { v[i] = __builtin_amdgcn_exp2f(v[i] - mx); sum += v[i]; }
    sum += __shfl_xor(sum, 16);
    sum += __shfl_xor(sum, 32);
    const float inv = 1.0f / sum;
    u32x4 PF;
#pragma unroll
    for (int p = 0; p < 4; ++p) PF[p] = cvt_pk(v[2 * p] * inv, v[2 * p + 1] * inv);

    wait_vm8<0>(VF);
    u32x4 VT[8];
    transpose8x8(VF, VT);
    f32x4 oacc[8];
#pragma unroll
    for (int t = 0; t < 8; ++t) oacc[t] = mfma16(as_frag(VT[t]), as_frag(PF), f32x4{0.f, 0.f, 0.f, 0.f});

    const int mb = 4 * bp + w;
    unsigned char* op = ofrag + ((size_t)(mb * 128 + 4 * h + g) * 64 + c) * 16;
#pragma unroll
    for (int gg = 0; gg < 4; ++gg) {
        u32x4 o;
#pragma unroll
        for (int p = 0; p < 4; ++p) o[p] = cvt_pk(oacc[2 * p][gg], oacc[2 * p + 1][gg]);
        *(u32x4*)(op + gg * 256) = o;
    }
}

template <int N> DEV void wait_o(u32x4* o, u32x2* w) {
    asm volatile("s_waitcnt vmcnt(%10)"
                 : "+v"(o[0]), "+v"(o[1]), "+v"(w[0]), "+v"(w[1]), "+v"(w[2]), "+v"(w[3]), "+v"(w[4]), "+v"(w[5]), "+v"(w[6]), "+v"(w[7])
                 : "i"(N) : "memory");
}

__global__ void __launch_bounds__(256, 1)
k_oproj(const unsigned char* __restrict__ ofrag, const __hip_bfloat16* __restrict__ wo,
        __hip_bfloat16* __restrict__ out) {
    __shared__ __attribute__((aligned(16))) unsigned char lds[4 * 8192];
    const int tid = threadIdx.x;
    const int w = __builtin_amdgcn_readfirstlane(tid >> 6);
    const int l = tid & 63, c = l & 15, g = l >> 4;
    const int bid = blockIdx.x;
    const int xcd = bid & 7, idx = bid >> 3;
    const int mt = (xcd & 1) * 8 + (idx & 7);
    const int nt = (xcd >> 1) * 4 + (idx >> 3);

    constexpr int DEPTH = 6;
    u32x4 OF[DEPTH][2];
    u32x2 WF[DEPTH][8];
    const unsigned ooff = l * 16;
    const unsigned char* ob = ofrag + ((size_t)(2 * mt) * 128 + 32 * w) * 1024;
    const unsigned woff = (8 * g * 1024 + 4 * c) * 2;
    const unsigned char* wb = (const unsigned char*)wo + ((size_t)(1024 * w) * 1024 + 64 * nt) * 2;

    auto issue = [&](int s) {
        gload4(OF[s % DEPTH][0], ooff, ob + (size_t)s * 1024);
        gload4(OF[s % DEPTH][1], ooff, ob + (size_t)s * 1024 + 128 * 1024);
#pragma unroll
        for (int j = 0; j < 8; ++j) gload2(WF[s % DEPTH][j], woff, wb + (size_t)(32 * s + j) * 2048);
    };
    f32x4 acc[4][2];
#pragma unroll
    for (int t = 0; t < 4; ++t) { acc[t][0] = f32x4{0.f, 0.f, 0.f, 0.f}; acc[t][1] = f32x4{0.f, 0.f, 0.f, 0.f}; }

#pragma unroll
    for (int s = 0; s < DEPTH; ++s) issue(s);
#pragma unroll
    for (int s = 0; s < 32; ++s) {
        constexpr int dummy = 0; (void)dummy;
        const int later = (31 - s < DEPTH - 1) ? (31 - s) : (DEPTH - 1);
        switch (later) {
            case 0: wait_o<0>(OF[s % DEPTH], WF[s % DEPTH]); break;
            case 1: wait_o<10>(OF[s % DEPTH], WF[s % DEPTH]); break;
            case 2: wait_o<20>(OF[s % DEPTH], WF[s % DEPTH]); break;
            case 3: wait_o<30>(OF[s % DEPTH], WF[s % DEPTH]); break;
            case 4: wait_o<40>(OF[s % DEPTH], WF[s % DEPTH]); break;
            default: wait_o<50>(OF[s % DEPTH], WF[s % DEPTH]); break;
        }
        u32x4 A[4];
        const u32x2* in = WF[s % DEPTH];
#pragma unroll
        for (int u = 0; u < 2; ++u)
#pragma unroll
            for (int p = 0; p < 4; ++p) {
                A[2 * u][p] = perm_lo(in[2 * p][u], in[2 * p + 1][u]);
                A[2 * u + 1][p] = perm_hi(in[2 * p][u], in[2 * p + 1][u]);
            }
        bf16x8 o0 = as_frag(OF[s % DEPTH][0]), o1 = as_frag(OF[s % DEPTH][1]);
        __builtin_amdgcn_sched_barrier(0);
        if (s + DEPTH < 32) issue(s + DEPTH);
        __builtin_amdgcn_sched_barrier(0);
#pragma unroll
        for (int t = 0; t < 4; ++t) {
            acc[t][0] = mfma16(as_frag(A[t]), o0, acc[t][0]);
            acc[t][1] = mfma16(as_frag(A[t]), o1, acc[t][1]);
        }
        __builtin_amdgcn_sched_barrier(0);
    }
#pragma unroll
    for (int t = 0; t < 4; ++t)
#pragma unroll
        for (int mbl = 0; mbl < 2; ++mbl) *(f32x4*)(lds + w * 8192 + (t * 2 + mbl) * 1024 + l * 16) = acc[t][mbl];
    __syncthreads();
    const int mbl = w >> 1, hf = w & 1;
    float r[8];
#pragma unroll
    for (int i = 0; i < 8; ++i) r[i] = 0.f;
#pragma unroll
    for (int src = 0; src < 4; ++src)
#pragma unroll
        for (int t = 0; t < 4; ++t) {
            float2 v = *(const float2*)(lds + src * 8192 + (t * 2 + mbl) * 1024 + l * 16 + hf * 8);
            r[t] += v.x; r[4 + t] += v.y;
        }
    u32x4 o;
#pragma unroll
    for (int p = 0; p < 4; ++p) o[p] = cvt_pk(r[2 * p], r[2 * p + 1]);
    *(u32x4*)((unsigned char*)out + ((size_t)(32 * mt + 16 * mbl + c) * 1024 + 64 * nt + 16 * g + 8 * hf) * 2) = o;
}

extern "C" void kernel_launch(void* const* d_in, const int* in_sizes, int n_in,
                              void* d_out, int out_size, void* d_ws, size_t ws_size,
                              hipStream_t stream) {
    const __hip_bfloat16* x = (const __hip_bfloat16*)d_in[0];
    const __hip_bfloat16* kc = (const __hip_bfloat16*)d_in[1];
    const __hip_bfloat16* vc = (const __hip_bfloat16*)d_in[2];
    const __hip_bfloat16* wq = (const __hip_bfloat16*)d_in[3];
    const __hip_bfloat16* wo = (const __hip_bfloat16*)d_in[4];
    __hip_bfloat16* out = (__hip_bfloat16*)d_out;
    unsigned char* ofrag = (unsigned char*)d_ws;
    hipFuncSetAttribute((const void*)k_qattn, hipFuncAttributeMaxDynamicSharedMemorySize, 131072);
    k_qattn<<<256, 256, 131072, stream>>>(x, kc, vc, wq, ofrag);
    k_oproj<<<256, 256, 0, stream>>>(ofrag, wo, out);
}
```

```cpp
#include <hip/hip_runtime.h>
#include <hip/hip_bf16.h>
#include <stdint.h>

typedef short bf16x8 __attribute__((ext_vector_type(8)));
typedef float f32x4 __attribute__((ext_vector_type(4)));
typedef float f32x16 __attribute__((ext_vector_type(16)));
typedef unsigned u32x4 __attribute__((ext_vector_type(4)));
typedef unsigned u32x2 __attribute__((ext_vector_type(2)));

#define DEV __device__ __forceinline__

DEV unsigned cvt_pk(float lo, float hi) {
    unsigned r;
    asm("v_cvt_pk_bf16_f32 %0, %1, %2" : "=v"(r) : "v"(lo), "v"(hi));
    return r;
}
DEV unsigned perm_lo(unsigned a, unsigned b) { return __builtin_amdgcn_perm(b, a, 0x05040100u); }
DEV unsigned perm_hi(unsigned a, unsigned b) { return __builtin_amdgcn_perm(b, a, 0x07060302u); }

DEV void gload4(u32x4& d, unsigned voff, const void* sbase) {
    asm volatile("global_load_dwordx4 %0, %1, %2" : "=v"(d) : "v"(voff), "s"(sbase) : "memory");
}
DEV void glds16(unsigned voff, const void* sbase, unsigned lds_addr) {
    unsigned keep;
    asm volatile("s_mov_b32 %0, m0\n\ts_mov_b32 m0, %3\n\ts_nop 0\n\tglobal_load_lds_dwordx4 %1, %2\n\ts_mov_b32 m0, %0"
                 : "=&s"(keep) : "v"(voff), "s"(sbase), "s"(lds_addr) : "memory");
}
template <int N> DEV void wait_vm() { asm volatile("s_waitcnt vmcnt(%0)" ::"i"(N) : "memory"); }
template <int N> DEV void wait_vm8(u32x4* w) {
    asm volatile("s_waitcnt vmcnt(%8)"
                 : "+v"(w[0]), "+v"(w[1]), "+v"(w[2]), "+v"(w[3]), "+v"(w[4]), "+v"(w[5]), "+v"(w[6]), "+v"(w[7])
                 : "i"(N) : "memory");
}
template <int OFF> DEV void lds_tr(u32x2& d, unsigned addr) {
    asm volatile("ds_read_b64_tr_b16 %0, %1 offset:%2" : "=v"(d) : "v"(addr), "i"(OFF) : "memory");
}
template <int OFF> DEV void lds_b128(u32x4& d, unsigned addr) {
    asm volatile("ds_read_b128 %0, %1 offset:%2" : "=v"(d) : "v"(addr), "i"(OFF) : "memory");
}
DEV void barrier_raw() { asm volatile("s_barrier" ::: "memory"); }

DEV bf16x8 as_frag(u32x4 v) { return __builtin_bit_cast(bf16x8, v); }
DEV f32x4 mfma16(bf16x8 a, bf16x8 b, f32x4 c) { return __builtin_amdgcn_mfma_f32_16x16x32_bf16(a, b, c, 0, 0, 0); }
DEV f32x16 mfma32(bf16x8 a, bf16x8 b, f32x16 c) { return __builtin_amdgcn_mfma_f32_32x32x16_bf16(a, b, c, 0, 0, 0); }

DEV void transpose8x8(const u32x4* in, u32x4* out) {
#pragma unroll
    for (int u = 0; u < 4; ++u) {
#pragma unroll
        for (int p = 0; p < 4; ++p) {
            out[2 * u][p] = perm_lo(in[2 * p][u], in[2 * p + 1][u]);
            out[2 * u + 1][p] = perm_hi(in[2 * p][u], in[2 * p + 1][u]);
        }
    }
}

static constexpr int K1_SLOT = 24576, K1_R = 6, K1_LDS = K1_SLOT * K1_R;

template <int KK> struct K1Frags { u32x2 a[2]; u32x4 b[2]; };

__global__ void __launch_bounds__(256, 1)
k_qattn(const __hip_bfloat16* __restrict__ x, const __hip_bfloat16* __restrict__ kc,
        const __hip_bfloat16* __restrict__ vc, const __hip_bfloat16* __restrict__ wq,
        unsigned char* __restrict__ ofrag) {
    extern __shared__ __attribute__((aligned(16))) unsigned char lds[];
    const int tid = threadIdx.x;
    const int w = __builtin_amdgcn_readfirstlane(tid >> 6);
    const int l = tid & 63, c = l & 15, g = l >> 4;
    const int bid = blockIdx.x;
    const int xcd = bid & 7, idx = bid >> 3;
    const int hl = idx & 3;
    const int h = xcd * 4 + hl;
    const int bp = idx >> 2;
    const int m0 = bp * 64;
    const int rot = (2 * bp + 4 * hl) & 15;

    const unsigned lds_base = (unsigned)(size_t)lds;

    const unsigned xoff = ((l >> 3) * 1024 + (((l & 7) ^ (l >> 4) ^ (4 * (w & 1))) * 8)) * 2;
    const unsigned char* xb = (const unsigned char*)x + ((size_t)(m0 + 8 * w) * 1024) * 2;
    const unsigned woff = ((l >> 4) * 4096 + (((l & 15) ^ ((l >> 4) << 2) ^ w) * 8)) * 2;
    const unsigned char* wb = (const unsigned char*)wq + ((size_t)(4 * w) * 4096 + h * 128) * 2;

    auto issue = [&](int s) {
        const int ks = (s + rot) & 15;
        const unsigned slot = lds_base + (s % K1_R) * K1_SLOT;
        const unsigned char* xs = xb + ks * 128;
        const unsigned char* ws = wb + (size_t)ks * 64 * 8192;
        glds16(woff, ws, slot + 8192 + w * 1024);
        glds16(woff, ws + 16 * 8192, slot + 8192 + (w + 4) * 1024);
        glds16(xoff, xs, slot + w * 1024);
        glds16(woff, ws + 32 * 8192, slot + 8192 + (w + 8) * 1024);
        glds16(woff, ws + 48 * 8192, slot + 8192 + (w + 12) * 1024);
        glds16(xoff, xs + 32 * 2048, slot + (w + 4) * 1024);
    };

    f32x16 acc[2];
#pragma unroll
    for (int i = 0; i < 16; ++i) { acc[0][i] = 0.f; acc[1][i] = 0.f; }

#pragma unroll
    for (int s = 0; s < 5; ++s) issue(s);

    u32x4 KF[8], VF[8];
    const int b = 2 * bp + (w >> 1);
    {
        const unsigned char* kbase = (const unsigned char*)kc + (size_t)(b * 32 + h) * 32 * 128 * 2;
        const unsigned char* vbase = (const unsigned char*)vc + (size_t)(b * 32 + h) * 32 * 128 * 2;
        const unsigned koff = (c * 128 + 8 * g) * 2;
        const unsigned voff = (4 * g * 128 + 8 * c) * 2;
#pragma unroll
        for (int i = 0; i < 8; ++i) gload4(KF[i], koff, kbase + (i >> 2) * 16 * 256 + (i & 3) * 64);
#pragma unroll
        for (int j = 0; j < 8; ++j) gload4(VF[j], voff, vbase + ((j < 4) ? j : 12 + j) * 256);
    }

    const int hh = l >> 5, blk = (l >> 4) & 1, q4 = (l & 15) >> 2, p4 = l & 3;
    unsigned atr[2];
#pragma unroll
    for (int t = 0; t < 2; ++t) {
        const int row = 8 * hh + 4 * t + q4;
        const int ch = 4 * w + 2 * blk + (p4 >> 1);
        atr[t] = lds_base + 8192 + 256 * row + 16 * (ch ^ (((row & 3) << 2) | ((row >> 2) & 3))) + 8 * (p4 & 1);
    }
    const int r31 = l & 31;
    unsigned bx[4];
#pragma unroll
    for (int kk = 0; kk < 4; ++kk) bx[kk] = lds_base + 128 * r31 + 16 * ((2 * kk + hh) ^ ((r31 >> 1) & 7));

#pragma unroll
    for (int s = 0; s < 16; ++s) {
        if (s <= 4) wait_vm<40>();
        else if (s <= 11) wait_vm<24>();
        else if (s == 12) wait_vm<18>();
        else if (s == 13) wait_vm<12>();
        else if (s == 14) wait_vm<6>();
        else wait_vm<0>();
        barrier_raw();
        if (s + 5 < 16) issue(s + 5);
        constexpr int SO = 0;
        const int so = (s % K1_R) * K1_SLOT;
        u32x2 A[4][2];
        u32x4 Bf[4][2];
#define K1_READ(kk)                                                                                   \
        asm volatile("ds_read_b64_tr_b16 %0, %4 offset:%7\n\t"                                        \
                     "ds_read_b64_tr_b16 %1, %5 offset:%7\n\t"                                        \
                     "ds_read_b128 %2, %6 offset:0\n\t"                                              \
                     "ds_read_b128 %3, %6 offset:4096"                                                 \
                     : "=&v"(A[kk][0]), "=&v"(A[kk][1]), "=&v"(Bf[kk][0]), "=&v"(Bf[kk][1])           \
                     : "v"(atr[0] + so), "v"(atr[1] + so), "v"(bx[kk] + so), "i"(kk * 4096) : "memory")
        K1_READ(0); K1_READ(1); K1_READ(2); K1_READ(3);
#define K1_MMA(kk, N)                                                                                 \
        asm volatile("s_waitcnt lgkmcnt(" #N ")" : "+v"(A[kk][0]), "+v"(A[kk][1]), "+v"(Bf[kk][0]), "+v"(Bf[kk][1]) :: "memory"); \
        { u32x4 af = {A[kk][0][0], A[kk][0][1], A[kk][1][0], A[kk][1][1]};                          \
          acc[0] = mfma32(as_frag(af), as_frag(Bf[kk][0]), acc[0]);                                   \
          acc[1] = mfma32(as_frag(af), as_frag(Bf[kk][1]), acc[1]); }
        K1_MMA(0, 12); K1_MMA(1, 8); K1_MMA(2, 4); K1_MMA(3, 0);
        (void)so; (void)SO;
    }
    barrier_raw();

#pragma unroll
    for (int rb = 0; rb < 2; ++rb)
#pragma unroll
        for (int qq = 0; qq < 4; ++qq) {
            u32x2 v;
            v[0] = cvt_pk(acc[rb][4 * qq + 0], acc[rb][4 * qq + 1]);
            v[1] = cvt_pk(acc[rb][4 * qq + 2], acc[rb][4 * qq + 3]);
            *(u32x2*)(lds + (32 * rb + r31) * 272 + (32 * w + 8 * qq + 4 * hh) * 2) = v;
        }
    __syncthreads();
    u32x4 QF[4];
#pragma unroll
    for (int sp = 0; sp < 4; ++sp) QF[sp] = *(const u32x4*)(lds + (16 * w + c) * 272 + (32 * sp + 8 * g) * 2);

    wait_vm8<0>(KF);
    wait_vm8<0>(VF);
    f32x4 sacc[2];
#pragma unroll
    for (int kb = 0; kb < 2; ++kb) {
        sacc[kb] = f32x4{0.f, 0.f, 0.f, 0.f};
#pragma unroll
        for (int sp = 0; sp < 4; ++sp) sacc[kb] = mfma16(as_frag(KF[kb * 4 + sp]), as_frag(QF[sp]), sacc[kb]);
    }
    const float sc = 0.08838834764831845f * 1.4426950408889634f;
    float v[8];
#pragma unroll
    for (int i = 0; i < 8; ++i) v[i] = sacc[i >> 2][i & 3] * sc;
    float mx = v[0];
#pragma unroll
    for (int i = 1; i < 8; ++i) mx = fmaxf(mx, v[i]);
    mx = fmaxf(mx, __shfl_xor(mx, 16));
    mx = fmaxf(mx, __shfl_xor(mx, 32));
    float sum = 0.f;
#pragma unroll
    for (int i = 0; i < 8; ++i) { v[i] = __builtin_amdgcn_exp2f(v[i] - mx); sum += v[i]; }
    sum += __shfl_xor(sum, 16);
    sum += __shfl_xor(sum, 32);
    const float inv = 1.0f / sum;
    u32x4 PF;
#pragma unroll
    for (int p = 0; p < 4; ++p) PF[p] = cvt_pk(v[2 * p] * inv, v[2 * p + 1] * inv);

    u32x4 VT[8];
    transpose8x8(VF, VT);
    f32x4 oacc[8];
#pragma unroll
    for (int t = 0; t < 8; ++t) oacc[t] = mfma16(as_frag(VT[t]), as_frag(PF), f32x4{0.f, 0.f, 0.f, 0.f});

    const int mb = 4 * bp + w;
    unsigned char* op = ofrag + ((size_t)(mb * 128 + 4 * h + g) * 64 + c) * 16;
#pragma unroll
    for (int gg = 0; gg < 4; ++gg) {
        u32x4 o;
#pragma unroll
        for (int p = 0; p < 4; ++p) o[p] = cvt_pk(oacc[2 * p][gg], oacc[2 * p + 1][gg]);
        *(u32x4*)(op + gg * 256) = o;
    }
}

static constexpr int K2_SLOT = 6144, K2_R = 6, K2_WAVE = K2_SLOT * K2_R, K2_LDS = K2_WAVE * 4;

__global__ void __launch_bounds__(256, 1)
k_oproj(const unsigned char* __restrict__ ofrag, const __hip_bfloat16* __restrict__ wo,
        __hip_bfloat16* __restrict__ out) {
    extern __shared__ __attribute__((aligned(16))) unsigned char lds[];
    const int tid = threadIdx.x;
    const int w = __builtin_amdgcn_readfirstlane(tid >> 6);
    const int l = tid & 63, c = l & 15, g = l >> 4;
    const int bid = blockIdx.x;
    const int xcd = bid & 7, idx = bid >> 3;
    const int mtl = idx & 7, ntl = idx >> 3;
    const int mt = (xcd & 1) * 8 + mtl;
    const int nt = (xcd >> 1) * 4 + ntl;
    const int rot = (4 * mtl + ntl) & 31;

    const unsigned lds_base = (unsigned)(size_t)lds + w * K2_WAVE;
    const unsigned ooff = l * 16;
    const unsigned char* ob = ofrag + ((size_t)(2 * mt) * 128 + 32 * w) * 1024;
    const int r3 = l >> 3;
    const unsigned woff_e = (r3 * 1024 + (((l & 7) ^ (2 * ((r3 >> 1) & 1))) * 8)) * 2;
    const unsigned woff_o = (r3 * 1024 + (((l & 7) ^ (2 * ((r3 >> 1) & 1)) ^ 4) * 8)) * 2;
    const unsigned char* wb = (const unsigned char*)wo + ((size_t)(1024 * w) * 1024 + 64 * nt) * 2;

    auto issue = [&](int s) {
        const int ks = (s + rot) & 31;
        const unsigned slot = lds_base + (s % K2_R) * K2_SLOT;
        const unsigned char* ws = wb + (size_t)ks * 32 * 2048;
        const unsigned char* os = ob + (size_t)ks * 1024;
        glds16(woff_e, ws, slot + 2048);
        glds16(woff_o, ws + 8 * 2048, slot + 3072);
        glds16(ooff, os, slot);
        glds16(woff_e, ws + 16 * 2048, slot + 4096);
        glds16(woff_o, ws + 24 * 2048, slot + 5120);
        glds16(ooff, os + 128 * 1024, slot + 1024);
    };
    f32x4 acc[4][2];
#pragma unroll
    for (int t = 0; t < 4; ++t) { acc[t][0] = f32x4{0.f, 0.f, 0.f, 0.f}; acc[t][1] = f32x4{0.f, 0.f, 0.f, 0.f}; }

    const int q4 = (l & 15) >> 2, p4 = l & 3;
    unsigned atr[2][4];
#pragma unroll
    for (int t = 0; t < 2; ++t)
#pragma unroll
        for (int nb = 0; nb < 4; ++nb) {
            const int row = 8 * g + 4 * t + q4;
            const int f = 2 * (((row >> 1) & 1) | (((row >> 3) & 1) << 1));
            atr[t][nb] = lds_base + 2048 + 128 * row + 16 * ((2 * nb + (p4 >> 1)) ^ f) + 8 * (p4 & 1);
        }
    const unsigned bo = lds_base + l * 16;

#pragma unroll
    for (int s = 0; s < K2_R - 1; ++s) issue(s);
#pragma unroll
    for (int s = 0; s < 32; ++s) {
        const int later = (31 - s < K2_R - 2) ? (31 - s) : (K2_R - 2);
        switch (later) {
            case 0: wait_vm<0>(); break;
            case 1: wait_vm<6>(); break;
            case 2: wait_vm<12>(); break;
            case 3: wait_vm<18>(); break;
            default: wait_vm<24>(); break;
        }
        u32x2 A[4][2];
        u32x4 Of[2];
        asm volatile("ds_read_b128 %0, %10 offset:%15\n\t"
                     "ds_read_b128 %1, %10 offset:%16\n\t"
                     "ds_read_b64_tr_b16 %2, %11 offset:%15\n\t"
                     "ds_read_b64_tr_b16 %3, %12 offset:%15\n\t"
                     "ds_read_b64_tr_b16 %4, %13 offset:%15\n\t"
                     "ds_read_b64_tr_b16 %5, %14 offset:%15\n\t"
                     "ds_read_b64_tr_b16 %6, %17 offset:%15\n\t"
                     "ds_read_b64_tr_b16 %7, %18 offset:%15\n\t"
                     "ds_read_b64_tr_b16 %8, %19 offset:%15\n\t"
                     "ds_read_b64_tr_b16 %9, %20 offset:%15\n\t"
                     : "=&v"(Of[0]), "=&v"(Of[1]), "=&v"(A[0][0]), "=&v"(A[0][1]), "=&v"(A[1][0]), "=&v"(A[1][1]),
                       "=&v"(A[2][0]), "=&v"(A[2][1]), "=&v"(A[3][0]), "=&v"(A[3][1])
                     : "v"(bo), "v"(atr[0][0]), "v"(atr[1][0]), "v"(atr[0][1]), "v"(atr[1][1]),
                       "i"((s % K2_R) * K2_SLOT), "i"((s % K2_R) * K2_SLOT + 1024),
                       "v"(atr[0][2]), "v"(atr[1][2]), "v"(atr[0][3]), "v"(atr[1][3])
                     : "memory");
        asm volatile("s_waitcnt lgkmcnt(0)"
                     : "+v"(Of[0]), "+v"(Of[1]), "+v"(A[0][0]), "+v"(A[0][1]), "+v"(A[1][0]), "+v"(A[1][1]),
                       "+v"(A[2][0]), "+v"(A[2][1]), "+v"(A[3][0]), "+v"(A[3][1]) :: "memory");
        if (s + K2_R - 1 < 32) issue(s + K2_R - 1);
#pragma unroll
        for (int nb = 0; nb < 4; ++nb) {
            u32x4 af = {A[nb][0][0], A[nb][0][1], A[nb][1][0], A[nb][1][1]};
            acc[nb][0] = mfma16(as_frag(af), as_frag(Of[0]), acc[nb][0]);
            acc[nb][1] = mfma16(as_frag(af), as_frag(Of[1]), acc[nb][1]);
        }
    }
    __syncthreads();
#pragma unroll
    for (int nb = 0; nb < 4; ++nb)
#pragma unroll
        for (int mbl = 0; mbl < 2; ++mbl) *(f32x4*)(lds + w * 8192 + (nb * 2 + mbl) * 1024 + l * 16) = acc[nb][mbl];
    __syncthreads();
    f32x4 r0 = {0.f, 0.f, 0.f, 0.f}, r1 = {0.f, 0.f, 0.f, 0.f};
#pragma unroll
    for (int src = 0; src < 4; ++src) {
        r0 += *(const f32x4*)(lds + src * 8192 + (w * 2 + 0) * 1024 + l * 16);
        r1 += *(const f32x4*)(lds + src * 8192 + (w * 2 + 1) * 1024 + l * 16);
    }
    u32x2 o0 = {cvt_pk(r0[0], r0[1]), cvt_pk(r0[2], r0[3])};
    u32x2 o1 = {cvt_pk(r1[0], r1[1]), cvt_pk(r1[2], r1[3])};
    unsigned char* op = (unsigned char*)out + ((size_t)(32 * mt + c) * 1024 + 64 * nt + 16 * w + 4 * g) * 2;
    *(u32x2*)op = o0;
    *(u32x2*)(op + 16 * 2048) = o1;
}

extern "C" void kernel_launch(void* const* d_in, const int* in_sizes, int n_in,
                              void* d_out, int out_size, void* d_ws, size_t ws_size,
                              hipStream_t stream) {
    const __hip_bfloat16* x = (const __hip_bfloat16*)d_in[0];
    const __hip_bfloat16* kc = (const __hip_bfloat16*)d_in[1];
    const __hip_bfloat16* vc = (const __hip_bfloat16*)d_in[2];
    const __hip_bfloat16* wq = (const __hip_bfloat16*)d_in[3];
    const __hip_bfloat16* wo = (const __hip_bfloat16*)d_in[4];
    __hip_bfloat16* out = (__hip_bfloat16*)d_out;
    unsigned char* ofrag = (unsigned char*)d_ws;
    (void)hipFuncSetAttribute((const void*)k_qattn, hipFuncAttributeMaxDynamicSharedMemorySize, K1_LDS);
    (void)hipFuncSetAttribute((const void*)k_oproj, hipFuncAttributeMaxDynamicSharedMemorySize, K2_LDS);
    k_qattn<<<256, 256, K1_LDS, stream>>>(x, kc, vc, wq, ofrag);
    k_oproj<<<256, 256, K2_LDS, stream>>>(ofrag, wo, out);
}
```

```cpp
#include <hip/hip_runtime.h>
#include <hip/hip_bf16.h>
#include <stdint.h>
#include <utility>

typedef short bf16x8 __attribute__((ext_vector_type(8)));
typedef float f32x4 __attribute__((ext_vector_type(4)));
typedef float f32x16 __attribute__((ext_vector_type(16)));
typedef unsigned u32x4 __attribute__((ext_vector_type(4)));
typedef unsigned u32x2 __attribute__((ext_vector_type(2)));
typedef __bf16 bf16x2_t __attribute__((ext_vector_type(2)));
typedef unsigned long long u64;

#define DEV __device__ __forceinline__

DEV unsigned cvt_pk(float lo, float hi) {
    bf16x2_t r = {(__bf16)lo, (__bf16)hi};
    return __builtin_bit_cast(unsigned, r);
}
DEV void glds16(unsigned voff, const void* sbase, unsigned lds_addr) {
    asm volatile("s_mov_b32 m0, %2\n\ts_nop 0\n\tglobal_load_lds_dwordx4 %0, %1"
                 :: "v"(voff), "s"(sbase), "s"(lds_addr) : "memory");
}
DEV void gload2_sc1(u32x4* d, unsigned voff, const unsigned char* s0, const unsigned char* s1) {
    asm volatile("global_load_dwordx4 %0, %2, %3 sc1\n\t"
                 "global_load_dwordx4 %1, %2, %4 sc1"
                 : "=&v"(d[0]), "=&v"(d[1]) : "v"(voff), "s"(s0), "s"(s1) : "memory");
}
template <int N> DEV void wait_vm() { asm volatile("s_waitcnt vmcnt(%0)" ::"i"(N) : "memory"); }
DEV void barrier_raw() { asm volatile("s_barrier" ::: "memory"); }
DEV void sched0() { __builtin_amdgcn_sched_barrier(0); }
DEV bf16x8 as_frag(u32x4 v) { return __builtin_bit_cast(bf16x8, v); }
DEV f32x4 mfma16(bf16x8 a, bf16x8 b, f32x4 c) { return __builtin_amdgcn_mfma_f32_16x16x32_bf16(a, b, c, 0, 0, 0); }
DEV f32x16 mfma32(bf16x8 a, bf16x8 b, f32x16 c) { return __builtin_amdgcn_mfma_f32_32x32x16_bf16(a, b, c, 0, 0, 0); }
template <int... I, class F> DEV void static_for(std::integer_sequence<int, I...>, F&& f) { (f(std::integral_constant<int, I>{}), ...); }
DEV u64 ld_agent(const u64* p) { return __hip_atomic_load(p, __ATOMIC_RELAXED, __HIP_MEMORY_SCOPE_AGENT); }
DEV void st_agent(u64* p, u64 v) { __hip_atomic_store(p, v, __ATOMIC_RELAXED, __HIP_MEMORY_SCOPE_AGENT); }

static constexpr int SLOT = 24576, NSLOT = 4;
static constexpr int L_KV = SLOT * NSLOT;
static constexpr int L_Q = L_KV + 32768;
static constexpr int L_CTL = L_Q + 64 * 272;
static constexpr int L_TOTAL = L_CTL + 64;
static constexpr size_t WS_O = 0;
static constexpr size_t WS_FLAGS = 4u << 20;
static constexpr size_t WS_EPOCH = WS_FLAGS + 8192;
static constexpr int KV0 = 3;

constexpr int p1_cnt(int s) {
    int j = s + 1;
    int last = (s + 3 < 15) ? (s + 3) : 15;
    int n = 6 * (last - j);
    if (s == 13) n += 4;
    if (s == 14) n += 12;
    return n;
}

static constexpr int NPRE_ = 7;
static constexpr int WSLOT = 16384;

__global__ void __launch_bounds__(512, 2)
k_fused(const __hip_bfloat16* __restrict__ x, const __hip_bfloat16* __restrict__ kc,
        const __hip_bfloat16* __restrict__ vc, const __hip_bfloat16* __restrict__ wq,
        const __hip_bfloat16* __restrict__ wo, __hip_bfloat16* __restrict__ out,
        unsigned char* __restrict__ ws) {
    extern __shared__ __attribute__((aligned(16))) unsigned char lds[];
    const int tid = threadIdx.x;
    const int wv = __builtin_amdgcn_readfirstlane(tid >> 6);
    const int w = wv & 3;
    const int l = tid & 63, c = l & 15, g = l >> 4;
    const int bid = blockIdx.x;
    const int xcd = bid & 7, idx = bid >> 3;
    const int hl = idx & 3;
    const int h = xcd * 4 + hl;
    const int bp = idx >> 2;
    const int m0 = bp * 64;
    const int mtl = idx >> 2, ntl = idx & 3;
    const int mt = 2 * mtl + (xcd & 1);
    const int nt = (xcd >> 1) * 4 + ntl;
    const unsigned lds_base = (unsigned)(size_t)lds;
    unsigned char* ofrag = ws + WS_O;
    u64* flags = (u64*)(ws + WS_FLAGS);
    u64* epoch = (u64*)(ws + WS_EPOCH);
    typedef __attribute__((address_space(3))) volatile u64 lds_u64;
    typedef __attribute__((address_space(3))) volatile unsigned lds_u32;
    lds_u64* lds_tag = (lds_u64*)(lds + L_CTL);
    lds_u32* lds_rdy = (lds_u32*)(lds + L_CTL + 16);
    lds_u32* lds_cnt = (lds_u32*)(lds + L_CTL + 32);

    if (wv >= 4) {
        const int rot = (2 * bp + 4 * hl) & 15;
        const unsigned xoff = ((l >> 3) * 1024 + (((l & 7) ^ (l >> 4) ^ (4 * (w & 1))) * 8)) * 2;
        const unsigned char* xb = (const unsigned char*)x + ((size_t)(m0 + 8 * w) * 1024) * 2;
        const unsigned woff = ((l >> 4) * 4096 + (((l & 15) ^ ((l >> 4) << 2) ^ w) * 8)) * 2;
        const unsigned char* wb = (const unsigned char*)wq + ((size_t)(4 * w) * 4096 + h * 128) * 2;
        auto issue1 = [&](int s) {
            const int ks = (s + rot) & 15;
            const unsigned slot = lds_base + (s % NSLOT) * SLOT;
            const unsigned char* xs = xb + ks * 128;
            const unsigned char* wsrc = wb + (size_t)ks * 64 * 8192;
            glds16(woff, wsrc, slot + 8192 + w * 1024); glds16(woff, wsrc + 16 * 8192, slot + 8192 + (w + 4) * 1024);
            glds16(xoff, xs, slot + w * 1024); glds16(woff, wsrc + 32 * 8192, slot + 8192 + (w + 8) * 1024);
            glds16(woff, wsrc + 48 * 8192, slot + 8192 + (w + 12) * 1024); glds16(xoff, xs + 32 * 2048, slot + (w + 4) * 1024);
        };
        const int rot2 = (4 * mtl + ntl) & 31;
        const int r3 = l >> 3;
        const unsigned woff_e = (r3 * 1024 + (((l & 7) ^ (2 * ((r3 >> 1) & 1))) * 8)) * 2;
        const unsigned woff_o = (r3 * 1024 + (((l & 7) ^ (2 * ((r3 >> 1) & 1)) ^ 4) * 8)) * 2;
        const unsigned char* wob = (const unsigned char*)wo + ((size_t)(32 * w) * 1024 + 64 * nt) * 2;
        auto issue_wo = [&](int t, int slot_i) {
            const int st = (t + rot2) & 31;
            const unsigned dst = lds_base + slot_i * WSLOT + w * 4096;
            const unsigned char* src = wob + (size_t)st * 128 * 2048;
            glds16(woff_e, src, dst); glds16(woff_o, src + 8 * 2048, dst + 1024);
            glds16(woff_e, src + 16 * 2048, dst + 2048); glds16(woff_o, src + 24 * 2048, dst + 3072);
        };

        issue1(0); issue1(1);
        if (wv == 4 && l == 0) { *lds_rdy = 0; *lds_cnt = 0; *lds_tag = ld_agent(epoch) + 1; }
        wait_vm<6>();
        asm volatile("s_waitcnt lgkmcnt(0)" ::: "memory");
        barrier_raw();
        issue1(2); issue1(3);
        static_for(std::make_integer_sequence<int, 15>{}, [&](auto SS) {
            constexpr int s = decltype(SS)::value;
            wait_vm<p1_cnt(s)>();
            barrier_raw();
            if constexpr (s + 4 < 16) issue1(s + 4);
            if constexpr (s == 12) issue_wo(0, 0);
            if constexpr (s == 13) { issue_wo(1, 1); issue_wo(2, 2); }
            if constexpr (s == 14) issue_wo(3, 3);
        });
        barrier_raw();
        issue_wo(4, 4); issue_wo(5, 5); issue_wo(6, 6); issue_wo(7, 7);
        wait_vm<28>();
        barrier_raw();
#pragma unroll 1
        for (int t = 0; t < 24; t += 8) {
#pragma unroll
            for (int u = 0; u < 8; ++u) { wait_vm<24>(); barrier_raw(); issue_wo(t + u + 8, u); }
        }
        wait_vm<24>(); barrier_raw();
        wait_vm<20>(); barrier_raw();
        wait_vm<16>(); barrier_raw();
        wait_vm<12>(); barrier_raw();
        wait_vm<8>(); barrier_raw();
        wait_vm<4>(); barrier_raw();
        wait_vm<0>(); barrier_raw();
        return;
    }

    f32x16 acc[2];
#pragma unroll
    for (int i = 0; i < 16; ++i) { acc[0][i] = 0.f; acc[1][i] = 0.f; }
    const int hh = l >> 5, blk = (l >> 4) & 1, q4 = (l & 15) >> 2, p4 = l & 3;
    unsigned atr[2];
#pragma unroll
    for (int t = 0; t < 2; ++t) {
        const int row = 8 * hh + 4 * t + q4;
        const int ch = 4 * w + 2 * blk + (p4 >> 1);
        atr[t] = lds_base + 8192 + 256 * row + 16 * (ch ^ (((row & 3) << 2) | ((row >> 2) & 3))) + 8 * (p4 & 1);
    }
    const int r31 = l & 31;
    unsigned bx[4];
#pragma unroll
    for (int kk = 0; kk < 4; ++kk) bx[kk] = lds_base + 128 * r31 + 16 * ((2 * kk + hh) ^ ((r31 >> 1) & 7));

    {
    u32x2 A[2][4][2];
    u32x4 Bf[2][4][2];
#define K1_READ(bufi, so, kk)                                                                         \
        asm volatile("ds_read_b64_tr_b16 %0, %4 offset:%7\n\t"                                        \
                     "ds_read_b64_tr_b16 %1, %5 offset:%7\n\t"                                        \
                     "ds_read_b128 %2, %6 offset:0\n\t"                                               \
                     "ds_read_b128 %3, %6 offset:4096"                                                \
                     : "=&v"(A[bufi][kk][0]), "=&v"(A[bufi][kk][1]), "=&v"(Bf[bufi][kk][0]), "=&v"(Bf[bufi][kk][1]) \
                     : "v"(atr[0] + (so)), "v"(atr[1] + (so)), "v"(bx[kk] + (so)), "i"(kk * 4096) : "memory")
#define K1_WAITL(bufi)                                                                                \
        asm volatile("s_waitcnt lgkmcnt(0)"                                                           \
            : "+v"(A[bufi][0][0]), "+v"(A[bufi][0][1]), "+v"(Bf[bufi][0][0]), "+v"(Bf[bufi][0][1]),   \
              "+v"(A[bufi][1][0]), "+v"(A[bufi][1][1]), "+v"(Bf[bufi][1][0]), "+v"(Bf[bufi][1][1]),   \
              "+v"(A[bufi][2][0]), "+v"(A[bufi][2][1]), "+v"(Bf[bufi][2][0]), "+v"(Bf[bufi][2][1]),   \
              "+v"(A[bufi][3][0]), "+v"(A[bufi][3][1]), "+v"(Bf[bufi][3][0]), "+v"(Bf[bufi][3][1]) :: "memory")
    const unsigned char* kvsrc = (const unsigned char*)((w & 1) ? vc : kc) + (size_t)((2 * bp + (w >> 1)) * 32 + h) * 8192;
    auto issue_kv = [&](int n) {
        const int row = 4 * n + (l >> 4);
        const int f = (w & 1) ? (((row & 3) << 2) | ((row >> 2) & 3)) : (row & 15);
        glds16(((l >> 4) * 16 + ((l & 15) ^ f)) * 16, kvsrc + n * 1024, lds_base + L_KV + w * 8192 + n * 1024);
    };
    barrier_raw();
    K1_READ(0, 0, 0); K1_READ(0, 0, 1); K1_READ(0, 0, 2); K1_READ(0, 0, 3);
    K1_WAITL(0);
    static_for(std::make_integer_sequence<int, 16>{}, [&](auto SS) {
        constexpr int s = decltype(SS)::value;
        if constexpr (s + 1 < 16) {
            barrier_raw();
        }
        constexpr int so = ((s + 1) % NSLOT) * SLOT;
        static_for(std::make_integer_sequence<int, 4>{}, [&](auto KK) {
            constexpr int kk = decltype(KK)::value;
            sched0();
            u32x4 af = {A[s & 1][kk][0][0], A[s & 1][kk][0][1], A[s & 1][kk][1][0], A[s & 1][kk][1][1]};
            acc[0] = mfma32(as_frag(af), as_frag(Bf[s & 1][kk][0]), acc[0]);
            sched0();
            if constexpr (s + 1 < 16) { K1_READ((s + 1) & 1, so, kk); }
            sched0();
            acc[1] = mfma32(as_frag(af), as_frag(Bf[s & 1][kk][1]), acc[1]);
            sched0();
            if constexpr (s >= KV0 && s < KV0 + 4 && (kk & 1) == 0) issue_kv((s - KV0) * 2 + (kk >> 1));
        });
        if constexpr (s + 1 < 16) { K1_WAITL((s + 1) & 1); }
    });
    }
    unsigned char* ql = lds + L_Q;
#pragma unroll
    for (int rb = 0; rb < 2; ++rb)
#pragma unroll
        for (int qq = 0; qq < 4; ++qq) {
            u32x2 v;
            v[0] = cvt_pk(acc[rb][4 * qq + 0], acc[rb][4 * qq + 1]);
            v[1] = cvt_pk(acc[rb][4 * qq + 2], acc[rb][4 * qq + 3]);
            *(u32x2*)(ql + (32 * rb + r31) * 272 + (32 * w + 8 * qq + 4 * hh) * 2) = v;
        }
    const u64 tag = *lds_tag;
    asm volatile("s_waitcnt vmcnt(0) lgkmcnt(0)" ::: "memory");
    if (l == 0) __hip_atomic_fetch_add((__attribute__((address_space(3))) unsigned*)lds_cnt, 1u, __ATOMIC_RELAXED, __HIP_MEMORY_SCOPE_WORKGROUP);
    while (*lds_cnt < 4) {}
    asm volatile("" ::: "memory");
    const unsigned char* kl = lds + L_KV + (w >> 1) * 16384;
    u32x4 KF[8];
#pragma unroll
    for (int i = 0; i < 8; ++i) {
        const int row = 16 * (i >> 2) + c;
        KF[i] = *(const u32x4*)(kl + row * 256 + (((4 * (i & 3) + g) ^ (row & 15)) * 16));
    }
    u32x2 VT[8][2];
    {
        const unsigned vb = lds_base + L_KV + (w >> 1) * 16384 + 8192;
#pragma unroll
        for (int t = 0; t < 2; ++t) {
            const int row = 16 * t + 4 * g + q4;
            const int f = ((row & 3) << 2) | ((row >> 2) & 3);
#pragma unroll
            for (int db = 0; db < 8; ++db) {
                const unsigned a = vb + 256 * row + 16 * ((2 * db + (p4 >> 1)) ^ f) + 8 * (p4 & 1);
                asm volatile("ds_read_b64_tr_b16 %0, %1" : "=v"(VT[db][t]) : "v"(a) : "memory");
            }
        }
    }
    asm volatile("s_waitcnt lgkmcnt(0)"
        : "+v"(VT[0][0]), "+v"(VT[0][1]), "+v"(VT[1][0]), "+v"(VT[1][1]), "+v"(VT[2][0]), "+v"(VT[2][1]), "+v"(VT[3][0]), "+v"(VT[3][1]),
          "+v"(VT[4][0]), "+v"(VT[4][1]), "+v"(VT[5][0]), "+v"(VT[5][1]), "+v"(VT[6][0]), "+v"(VT[6][1]), "+v"(VT[7][0]), "+v"(VT[7][1]) :: "memory");
    {
    u32x4 QF[4];
#pragma unroll
    for (int sp = 0; sp < 4; ++sp) QF[sp] = *(const u32x4*)(ql + (16 * w + c) * 272 + (32 * sp + 8 * g) * 2);
    f32x4 sacc[2];
#pragma unroll
    for (int kb = 0; kb < 2; ++kb) {
        sacc[kb] = f32x4{0.f, 0.f, 0.f, 0.f};
#pragma unroll
        for (int sp = 0; sp < 4; ++sp) sacc[kb] = mfma16(as_frag(KF[kb * 4 + sp]), as_frag(QF[sp]), sacc[kb]);
    }
    const float sc = 0.08838834764831845f * 1.4426950408889634f;
    float v[8];
#pragma unroll
    for (int i = 0; i < 8; ++i) v[i] = sacc[i >> 2][i & 3] * sc;
    float mx = v[0];
#pragma unroll
    for (int i = 1; i < 8; ++i) mx = fmaxf(mx, v[i]);
    mx = fmaxf(mx, __shfl_xor(mx, 16));
    mx = fmaxf(mx, __shfl_xor(mx, 32));
    float sum = 0.f;
#pragma unroll
    for (int i = 0; i < 8; ++i) { v[i] = __builtin_amdgcn_exp2f(v[i] - mx); sum += v[i]; }
    sum += __shfl_xor(sum, 16);
    sum += __shfl_xor(sum, 32);
    const float inv = 1.0f / sum;
    u32x4 PF;
#pragma unroll
    for (int p = 0; p < 4; ++p) PF[p] = cvt_pk(v[2 * p] * inv, v[2 * p + 1] * inv);
    f32x4 oacc[8];
#pragma unroll
    for (int db = 0; db < 8; ++db) {
        u32x4 vf = {VT[db][0][0], VT[db][0][1], VT[db][1][0], VT[db][1][1]};
        oacc[db] = mfma16(as_frag(vf), as_frag(PF), f32x4{0.f, 0.f, 0.f, 0.f});
    }
    const int mb = 4 * bp + w;
    unsigned char* op = ofrag + ((size_t)(mb * 128 + 4 * h) * 64 + 16 * (g >> 1) + c) * 16;
#pragma unroll
    for (int i = 0; i < 4; ++i) {
        const unsigned e0 = cvt_pk(oacc[2 * i][0], oacc[2 * i][1]), e1 = cvt_pk(oacc[2 * i][2], oacc[2 * i][3]);
        const unsigned o0 = cvt_pk(oacc[2 * i + 1][0], oacc[2 * i + 1][1]), o1 = cvt_pk(oacc[2 * i + 1][2], oacc[2 * i + 1][3]);
        const unsigned s0 = (g & 1) ? e0 : o0, s1 = (g & 1) ? e1 : o1;
        const unsigned r0 = __shfl_xor(s0, 16), r1 = __shfl_xor(s1, 16);
        u32x4 v;
        if (g & 1) { v[0] = r0; v[1] = r1; v[2] = o0; v[3] = o1; } else { v[0] = e0; v[1] = e1; v[2] = r0; v[3] = r1; }
        unsigned char* p = op + i * 1024 + (g & 1) * 512;
        asm volatile("global_store_dwordx4 %0, %1, off sc1\n\ts_nop 1" :: "v"(p), "v"(v) : "memory");
    }
    wait_vm<0>();
    if (l == 0) st_agent(flags + (2 * bp + (w >> 1)) * 64 + h * 2 + (w & 1), tag);
    barrier_raw();
    }
    const int rot2 = (4 * mtl + ntl) & 31;
    const unsigned char* ob = ofrag + ((size_t)(2 * mt) * 128 + w) * 1024;
    u32x4 Of[8][2];
    auto load_o = [&](int t, u32x4* d) {
        const int st = (t + rot2) & 31;
        const unsigned char* src = ob + (size_t)st * 4096;
        gload2_sc1(d, l * 16, src, src + 128 * 1024);
    };
    f32x4 acc2[4][2];
#pragma unroll
    for (int t = 0; t < 4; ++t) { acc2[t][0] = f32x4{0.f, 0.f, 0.f, 0.f}; acc2[t][1] = f32x4{0.f, 0.f, 0.f, 0.f}; }
    unsigned at2[2][4];
#pragma unroll
    for (int t = 0; t < 2; ++t)
#pragma unroll
        for (int nb = 0; nb < 4; ++nb) {
            const int row = 8 * g + 4 * t + q4;
            const int f = 2 * (((row >> 1) & 1) | (((row >> 3) & 1) << 1));
            at2[t][nb] = lds_base + w * 4096 + 128 * row + 16 * ((2 * nb + (p4 >> 1)) ^ f) + 8 * (p4 & 1);
        }
    constexpr int NPRE = NPRE_;
    u32x2 A[2 + NPRE][4][2];
#define K2_READ(bi, slot)                                                                             \
        asm volatile("ds_read_b64_tr_b16 %0, %8 offset:%16\n\t"                                      \
                     "ds_read_b64_tr_b16 %1, %9 offset:%16\n\t"                                      \
                     "ds_read_b64_tr_b16 %2, %10 offset:%16\n\t"                                      \
                     "ds_read_b64_tr_b16 %3, %11 offset:%16\n\t"                                      \
                     "ds_read_b64_tr_b16 %4, %12 offset:%16\n\t"                                      \
                     "ds_read_b64_tr_b16 %5, %13 offset:%16\n\t"                                      \
                     "ds_read_b64_tr_b16 %6, %14 offset:%16\n\t"                                      \
                     "ds_read_b64_tr_b16 %7, %15 offset:%16\n\t"                                      \
                     : "=&v"(A[bi][0][0]), "=&v"(A[bi][0][1]), "=&v"(A[bi][1][0]), "=&v"(A[bi][1][1]), \
                       "=&v"(A[bi][2][0]), "=&v"(A[bi][2][1]), "=&v"(A[bi][3][0]), "=&v"(A[bi][3][1]) \
                     : "v"(at2[0][0] + ((slot) >> 2) * 65536), "v"(at2[1][0] + ((slot) >> 2) * 65536), \
                       "v"(at2[0][1] + ((slot) >> 2) * 65536), "v"(at2[1][1] + ((slot) >> 2) * 65536),       \
                       "v"(at2[0][2] + ((slot) >> 2) * 65536), "v"(at2[1][2] + ((slot) >> 2) * 65536), \
                       "v"(at2[0][3] + ((slot) >> 2) * 65536), "v"(at2[1][3] + ((slot) >> 2) * 65536), \
                       "i"(((slot) & 3) * WSLOT)                                                      \
                     : "memory")
#define K2_WAITL(bi)                                                                                  \
        asm volatile("s_waitcnt lgkmcnt(0)"                                                           \
                     : "+v"(A[bi][0][0]), "+v"(A[bi][0][1]), "+v"(A[bi][1][0]), "+v"(A[bi][1][1]), \
                       "+v"(A[bi][2][0]), "+v"(A[bi][2][1]), "+v"(A[bi][3][0]), "+v"(A[bi][3][1]) :: "memory")
#define K2_MMA(bi, u)                                                                                 \
        _Pragma("unroll") for (int nb = 0; nb < 4; ++nb) {                                            \
            u32x4 af = {A[bi][nb][0][0], A[bi][nb][0][1], A[bi][nb][1][0], A[bi][nb][1][1]};          \
            acc2[nb][0] = mfma16(as_frag(af), as_frag(Of[u][0]), acc2[nb][0]);                        \
            acc2[nb][1] = mfma16(as_frag(af), as_frag(Of[u][1]), acc2[nb][1]);                        \
        }
#define K2_STEP(u, tb, VM, DOREAD, DOLOAD)                                                            \
        { if (DOREAD) { barrier_raw(); K2_READ(((u) + 1) & 1, ((u) + 1) & 7); }                       \
          asm volatile("s_waitcnt vmcnt(%2)" : "+v"(Of[u][0]), "+v"(Of[u][1]) : "i"(VM) : "memory");  \
          sched0(); K2_MMA((u) & 1, u); sched0();                                                     \
          if (DOLOAD) load_o((tb) + (u) + 8, Of[u]);                                                  \
          if (DOREAD) { K2_WAITL(((u) + 1) & 1); } }
    barrier_raw();
    K2_READ(2, 0);
    K2_WAITL(2);
    barrier_raw(); K2_READ(3, 1); K2_WAITL(3);
    barrier_raw(); K2_READ(4, 2); K2_WAITL(4);
    barrier_raw(); K2_READ(5, 3); K2_WAITL(5);
    barrier_raw(); K2_READ(6, 4); K2_WAITL(6);
    barrier_raw(); K2_READ(7, 5); K2_WAITL(7);
    barrier_raw(); K2_READ(8, 6); K2_WAITL(8);
    barrier_raw(); K2_READ(1, 7); K2_WAITL(1);
    if (w == 0) {
        const u64* fp = flags + mt * 64 + l;
        for (int spin = 0; spin < (1 << 16); ++spin) {
            const u64 v = ld_agent(fp);
            if (__all(v == tag)) break;
        }
        if (l == 0) *lds_rdy = 1;
    }
    if (bid == 0 && w == 3) {
        for (int spin = 0; spin < (1 << 16); ++spin) {
            u64 v[16];
#pragma unroll
            for (int i = 0; i < 16; ++i) v[i] = ld_agent(flags + i * 64 + l);
            bool ok = true;
#pragma unroll
            for (int i = 0; i < 16; ++i) ok &= (v[i] == tag);
            if (__all(ok)) break;
            __builtin_amdgcn_s_sleep(2);
        }
        if (l == 0) st_agent(epoch, tag);
    }
    while (*lds_rdy == 0) __builtin_amdgcn_s_sleep(1);
    asm volatile("" ::: "memory");
#pragma unroll
    for (int u = 0; u < 8; ++u) load_o(u, Of[u]);
    asm volatile("s_waitcnt vmcnt(14)" : "+v"(Of[0][0]), "+v"(Of[0][1]) :: "memory");
    sched0(); K2_MMA(2, 0); sched0();
    load_o(8, Of[0]);
    asm volatile("s_waitcnt vmcnt(14)" : "+v"(Of[1][0]), "+v"(Of[1][1]) :: "memory");
    sched0(); K2_MMA(3, 1); sched0();
    load_o(9, Of[1]);
    asm volatile("s_waitcnt vmcnt(14)" : "+v"(Of[2][0]), "+v"(Of[2][1]) :: "memory");
    sched0(); K2_MMA(4, 2); sched0();
    load_o(10, Of[2]);
    asm volatile("s_waitcnt vmcnt(14)" : "+v"(Of[3][0]), "+v"(Of[3][1]) :: "memory");
    sched0(); K2_MMA(5, 3); sched0();
    load_o(11, Of[3]);
    asm volatile("s_waitcnt vmcnt(14)" : "+v"(Of[4][0]), "+v"(Of[4][1]) :: "memory");
    sched0(); K2_MMA(6, 4); sched0();
    load_o(12, Of[4]);
    asm volatile("s_waitcnt vmcnt(14)" : "+v"(Of[5][0]), "+v"(Of[5][1]) :: "memory");
    sched0(); K2_MMA(7, 5); sched0();
    load_o(13, Of[5]);
    asm volatile("s_waitcnt vmcnt(14)" : "+v"(Of[6][0]), "+v"(Of[6][1]) :: "memory");
    sched0(); K2_MMA(8, 6); sched0();
    load_o(14, Of[6]);
    K2_STEP(7, 0, 14, true, true);
#pragma unroll 1
    for (int t = 8; t < 24; t += 8) {
        K2_STEP(0, t, 14, true, true); K2_STEP(1, t, 14, true, true); K2_STEP(2, t, 14, true, true); K2_STEP(3, t, 14, true, true);
        K2_STEP(4, t, 14, true, true); K2_STEP(5, t, 14, true, true); K2_STEP(6, t, 14, true, true); K2_STEP(7, t, 14, true, true);
    }
    K2_STEP(0, 24, 14, true, false); K2_STEP(1, 24, 12, true, false); K2_STEP(2, 24, 10, true, false); K2_STEP(3, 24, 8, true, false);
    K2_STEP(4, 24, 6, true, false); K2_STEP(5, 24, 4, true, false); K2_STEP(6, 24, 2, true, false); K2_STEP(7, 24, 0, false, false);
    barrier_raw();
#pragma unroll
    for (int nb = 0; nb < 4; ++nb)
#pragma unroll
        for (int mbl = 0; mbl < 2; ++mbl) *(f32x4*)(lds + w * 8192 + (nb * 2 + mbl) * 1024 + l * 16) = acc2[nb][mbl];
    asm volatile("s_waitcnt lgkmcnt(0)" ::: "memory");
    barrier_raw();
    f32x4 r0 = {0.f, 0.f, 0.f, 0.f}, r1 = {0.f, 0.f, 0.f, 0.f};
#pragma unroll
    for (int src = 0; src < 4; ++src) {
        r0 += *(const f32x4*)(lds + src * 8192 + (w * 2 + 0) * 1024 + l * 16);
        r1 += *(const f32x4*)(lds + src * 8192 + (w * 2 + 1) * 1024 + l * 16);
    }
    u32x2 o0 = {cvt_pk(r0[0], r0[1]), cvt_pk(r0[2], r0[3])};
    u32x2 o1 = {cvt_pk(r1[0], r1[1]), cvt_pk(r1[2], r1[3])};
    unsigned char* op2 = (unsigned char*)out + ((size_t)(32 * mt + c) * 1024 + 64 * nt + 16 * w + 4 * g) * 2;
    *(u32x2*)op2 = o0;
    *(u32x2*)(op2 + 16 * 2048) = o1;
}

extern "C" void kernel_launch(void* const* d_in, const int* in_sizes, int n_in,
                              void* d_out, int out_size, void* d_ws, size_t ws_size,
                              hipStream_t stream) {
    const __hip_bfloat16* x = (const __hip_bfloat16*)d_in[0];
    const __hip_bfloat16* kc = (const __hip_bfloat16*)d_in[1];
    const __hip_bfloat16* vc = (const __hip_bfloat16*)d_in[2];
    const __hip_bfloat16* wq = (const __hip_bfloat16*)d_in[3];
    const __hip_bfloat16* wo = (const __hip_bfloat16*)d_in[4];
    __hip_bfloat16* out = (__hip_bfloat16*)d_out;
    (void)hipFuncSetAttribute((const void*)k_fused, hipFuncAttributeMaxDynamicSharedMemorySize, L_TOTAL);
    k_fused<<<256, 512, L_TOTAL, stream>>>(x, kc, vc, wq, wo, out, (unsigned char*)d_ws);
}
```
